```python
import torch

def _softmax(x, axis=-1):
    return torch.softmax(x, dim=axis)
def _gelu(x):
    return torch.nn.functional.gelu(x, approximate="tanh")
def _silu(x):
    return torch.nn.functional.silu(x)
def _sigmoid(x):
    return torch.sigmoid(x)
def _layernorm(x, axis=-1, eps=1e-5):
    m = x.mean(dim=axis, keepdim=True)
    v = x.var(dim=axis, keepdim=True, unbiased=False)
    return (x - m) / torch.sqrt(v + eps)
def _scatter_add(tgt, idx, src, axis=0):
    return tgt.clone().index_add_(axis, idx, src)
def _segment_sum(src, seg, n):
    out = torch.zeros((n,) + src.shape[1:], dtype=src.dtype)
    return out.index_add_(0, seg, src)
def _pad(x, pw):
    flat = tuple(v for pair in reversed(pw) for v in pair)
    return torch.nn.functional.pad(x, flat)

kernel_name = 'parallel_sum__linear_attn_block__dense_activation_relu__prompt'

def setup_inputs():

    _w = (lambda a: (lambda t: t.to(torch.bfloat16) if t.is_floating_point() else t)(torch.as_tensor(a)))
    def _A_setup():
        B, S, D = 1, 2048, 640
        return {
            "x": _w((lambda *s: torch.randn(*s, dtype=torch.bfloat16) * 0.05)(B, S, D)),
            "wqkv": _w((lambda *s: torch.randn(*s, dtype=torch.bfloat16) * 0.2)(D, 3 * D)),
            "wo": _w((lambda *s: torch.randn(*s, dtype=torch.bfloat16) * 0.2)(D, D)),
        }
    _A_in = _A_setup()

    def _B_setup():
        B, S, D, Dff = 1, 2048, 640, 1536
        return {
            "x": _w((lambda *s: torch.randn(*s, dtype=torch.bfloat16) * 0.05)(B, S, D)),
            "w1": _w((lambda *s: torch.randn(*s, dtype=torch.bfloat16) * 0.05)(D, Dff)),
            "w2": _w((lambda *s: torch.randn(*s, dtype=torch.bfloat16) * 0.05)(Dff, D)),
        }
    _B_in = _B_setup()

    return {
        "A_x": _A_in["x"],
        "A_wqkv": _A_in["wqkv"],
        "A_wo": _A_in["wo"],
        "B_x": _B_in["x"],
        "B_w1": _B_in["w1"],
        "B_w2": _B_in["w2"],
    }

def reference(A_x, A_wqkv, A_wo, B_x, B_w1, B_w2):
    def _A_ref(x, wqkv, wo):
        B, S, D = x.shape
        H = 32; Dh = D // H
        qkv = (lambda x, *d: x.permute(*d))((x @ wqkv).reshape(B, S, 3, H, Dh), 2, 0, 3, 1, 4)
        q, k, v = qkv[0], qkv[1], qkv[2]
        phi = lambda t: torch.where(t > 0, t + 1, torch.exp(t)) + 1e-6
        pq, pk = phi(q), phi(k)
        kv = torch.einsum("bhsd,bhse->bhde", pk, v)
        z = torch.einsum("bhsd->bhd", pk)
        num = torch.einsum("bhqd,bhde->bhqe", pq, kv)
        den = torch.einsum("bhqd,bhd->bhq", pq, z)[..., None] + 1e-6
        o = num / den
        return (lambda x, *d: x.permute(*d))(o, 0, 2, 1, 3).reshape(B, S, D) @ wo

    def _B_ref(x, w1, w2):
        h = torch.einsum("bsd,df->bsf", x, w1)
        zero = torch.zeros_like(h)
        h = torch.maximum(h, zero)
        return torch.einsum("bsf,fd->bsd", h, w2)

    x = A_x
    xn = _layernorm(x, axis=-1)
    return x + _A_ref(xn, A_wqkv, A_wo) + _B_ref(xn, B_w1, B_w2)
```

```cpp
#include <hip/hip_runtime.h>
#include <hip/hip_bf16.h>
#include <stdint.h>

typedef __attribute__((ext_vector_type(8))) __bf16 bf16x8;
typedef __attribute__((ext_vector_type(4))) __bf16 bf16x4;
typedef __attribute__((ext_vector_type(2))) __bf16 bf16x2;
typedef __attribute__((ext_vector_type(4))) float f32x4;
typedef __attribute__((ext_vector_type(2))) float f32x2;
typedef __attribute__((ext_vector_type(4))) unsigned u32x4;
typedef __attribute__((ext_vector_type(2))) unsigned u32x2;
#define LDS3 __attribute__((address_space(3)))
#define GL1 __attribute__((address_space(1)))

constexpr unsigned WS_H = 0;
constexpr unsigned WS_PQ = WS_H + 128u * 48u * 1024u;
constexpr unsigned WS_WF = WS_PQ + 128u * 32u * 640u + 1024u;
constexpr unsigned WS_P = WS_WF + 8u * 68u * 5u * 1024u;
constexpr unsigned WS_F = WS_P + 32u * 21u * 5u * 16u * 16u;
constexpr unsigned WS_CNT = WS_F + 32u * 1024u;

constexpr int A_BYTES = 128 * 128;
constexpr int B_ROW = 448;
constexpr int B_BYTES = 64 * B_ROW;
constexpr int STAGE = A_BYTES + B_BYTES;
constexpr int L_SCR = 3 * STAGE;
constexpr int L_STAT = L_SCR + 3 * 4096;
constexpr int K1_LDS = L_STAT + (256 + 448) * 4;
constexpr int K2_LDS = 4 * 15 * 1024;

__device__ __forceinline__ f32x4 mfma16(bf16x8 a, bf16x8 b, f32x4 c) {
  return __builtin_amdgcn_mfma_f32_16x16x32_bf16(a, b, c, 0, 0, 0);
}
__device__ __forceinline__ unsigned pk2(float a, float b) {
  f32x2 p = {a, b};
  return __builtin_bit_cast(unsigned, __builtin_convertvector(p, bf16x2));
}
__device__ __forceinline__ float phi_f(float t) {
  return (t > 0.f ? t + 1.f : __expf(t)) + 1e-6f;
}
__device__ __forceinline__ void glds16(const char* g, unsigned lds) {
  unsigned keep;
  asm volatile("s_mov_b32 %0, m0\n\ts_mov_b32 m0, %2\n\ts_nop 0\n\tglobal_load_lds_dwordx4 %1, off\n\ts_mov_b32 m0, %0"
               : "=&s"(keep)
               : "v"(g), "s"(lds)
               : "memory");
}
__device__ __forceinline__ bf16x8 lds_b128(const char* smem, unsigned off) {
  return __builtin_bit_cast(bf16x8, *(const LDS3 u32x4*)((const LDS3 char*)smem + off));
}
__device__ __forceinline__ bf16x8 lds_tr2(const char* smem, unsigned off0, unsigned off1) {
  bf16x4 lo = __builtin_amdgcn_ds_read_tr16_b64_v4bf16((LDS3 bf16x4*)((LDS3 char*)smem + off0));
  bf16x4 hi = __builtin_amdgcn_ds_read_tr16_b64_v4bf16((LDS3 bf16x4*)((LDS3 char*)smem + off1));
  return __builtin_shufflevector(lo, hi, 0, 1, 2, 3, 4, 5, 6, 7);
}

struct Frags {
  bf16x8 a[2];
  bf16x8 b[7];
};

template <bool KV>
__device__ __forceinline__ void k1_body(const char* __restrict__ x, const char* __restrict__ wqkv,
                                        const char* __restrict__ w1, const char* __restrict__ wo,
                                        const char* __restrict__ w2, char* __restrict__ ws, char* smem, int rb, int grp,
                                        int rot) {
  const int tid = threadIdx.x;
  const int w = __builtin_amdgcn_readfirstlane(tid >> 6);
  const int l = tid & 63;
  const int mw = w & 3, nw = w >> 2;
  const int r16 = l & 15, g = l >> 4;
  const unsigned lbase = __builtin_amdgcn_readfirstlane((unsigned)(uintptr_t)(LDS3 char*)smem);

  if (blockIdx.x == 0 && tid < 32) *(unsigned*)(ws + WS_CNT + tid * 128) = 0u;

  const int task = blockIdx.x * 3 + w;
  const bool has_task = (w < 3) && (task < 680);
  if (has_task) {
    const int T = task / 10, c = task % 10;
    const int w4 = T / 17, tt = T % 17;
#pragma unroll
    for (int pc = 0; pc < 4; ++pc) {
      const int rho = pc * 8 + (l >> 3);
      const char* src;
      if (tt < 12) {
        src = w2 + (size_t)((12 * w4 + tt) * 32 + rho) * 1280;
      } else {
        const int j = (rho >> 4) * 4 + (rho & 3), gg = (rho & 15) >> 2;
        const int L = 8 * (tt - 12) + j;
        src = wo + (size_t)(160 * w4 + 20 * (L / 5) + 5 * gg + (L % 5)) * 1280;
      }
      glds16(src + c * 128 + (l & 7) * 16, lbase + L_SCR + w * 4096 + pc * 1024);
    }
  }

  const char* pa[2];
#pragma unroll
  for (int i = 0; i < 2; ++i) {
    const int row = (w + 8 * i) * 8 + (l >> 3);
    pa[i] = x + (size_t)(rb * 128 + row) * 1280 + (((l & 7) ^ (row & 7)) * 16);
  }
  const char* pb[4];
  unsigned sb[4];
#pragma unroll
  for (int i = 0; i < 4; ++i) {
    int L = (w + 8 * i) * 64 + l;
    if (L >= 1792) L -= 512;
    const int row = L / 28, cp = L % 28;
    const int c = cp ^ (((row >> 3) & 1) << 1);
    if (KV) {
      int col;
      if (c < 10) col = 640 + 80 * grp + 8 * c;
      else if (c < 14) col = 64 * grp + 8 * (c - 10);
      else if (c < 24) col = 1280 + 80 * grp + 8 * (c - 14);
      else col = 64 * grp + 32 + 8 * (c - 24);
      pb[i] = wqkv + (size_t)row * 3840 + col * 2;
      sb[i] = 64 * 3840;
    } else {
      if (c < 2) {
        pb[i] = wqkv + (size_t)row * 3840 + (512 + 16 * grp + 8 * c) * 2;
        sb[i] = 64 * 3840;
      } else {
        const int cc = c < 26 ? c : c - 2;
        pb[i] = w1 + (size_t)row * 3072 + (192 * grp + 8 * (cc - 2)) * 2;
        sb[i] = 64 * 3072;
      }
    }
  }
  auto issue_half0 = [&](int st) {
    int kb = st + rot;
    if (kb >= 10) kb -= 10;
    const unsigned slot = lbase + (st % 3) * STAGE;
    glds16(pa[0] + kb * 128, slot + w * 1024);
    glds16(pb[0] + (size_t)((unsigned)kb * sb[0]), slot + A_BYTES + w * 1024);
    glds16(pb[1] + (size_t)((unsigned)kb * sb[1]), slot + A_BYTES + (w + 8) * 1024);
  };
  auto issue_half1 = [&](int st) {
    int kb = st + rot;
    if (kb >= 10) kb -= 10;
    const unsigned slot = lbase + (st % 3) * STAGE;
    glds16(pa[1] + kb * 128, slot + (w + 8) * 1024);
    glds16(pb[2] + (size_t)((unsigned)kb * sb[2]), slot + A_BYTES + (w + 16) * 1024);
    if (w < 4) glds16(pb[3] + (size_t)((unsigned)kb * sb[3]), slot + A_BYTES + (w + 24) * 1024);
  };
  issue_half0(0); issue_half1(0);
  issue_half0(1); issue_half1(1);
  issue_half0(2); issue_half1(2);

  unsigned aoff[2];
#pragma unroll
  for (int m = 0; m < 2; ++m) {
    const int row = 32 * mw + 16 * (m ^ nw) + r16;
    aoff[m] = row * 128 + ((g ^ (r16 & 7)) * 16);
  }
  const unsigned brow = A_BYTES + (8 * g + (r16 >> 2)) * B_ROW + 8 * (r16 & 3) + nw * 224;
  const int flip = (g & 1) * 32;
  const unsigned bev = brow + (nw ? -flip : flip);
  const unsigned bod = brow + (nw ? flip : -flip);

  auto load_frags = [&](Frags& f, unsigned slot_off, int ks) {
#pragma unroll
    for (int m = 0; m < 2; ++m) f.a[m] = lds_b128(smem, slot_off + (aoff[m] ^ (ks * 64)));
#pragma unroll
    for (int u = 0; u < 7; ++u) {
      const unsigned o = slot_off + ((u & 1) ? bod : bev) + u * 32 + ks * 32 * B_ROW;
      f.b[u] = lds_tr2(smem, o, o + 4 * B_ROW);
    }
  };

  f32x4 acc[2][7];
#pragma unroll
  for (int m = 0; m < 2; ++m)
#pragma unroll
    for (int u = 0; u < 7; ++u) acc[m][u] = f32x4{0.f, 0.f, 0.f, 0.f};
  f32x4 gram = {0.f, 0.f, 0.f, 0.f}, rsum = {0.f, 0.f, 0.f, 0.f};
  f32x4 csum[4];
#pragma unroll
  for (int i = 0; i < 4; ++i) csum[i] = f32x4{0.f, 0.f, 0.f, 0.f};
  const bf16x8 ones = __builtin_bit_cast(bf16x8, u32x4{0x3F803F80u, 0x3F803F80u, 0x3F803F80u, 0x3F803F80u});

  auto mma = [&](const Frags& f, int ks) {
#pragma unroll
    for (int u = 0; u < 7; ++u)
#pragma unroll
      for (int m = 0; m < 2; ++m) {
        if (KV && u < 5) acc[m][u] = mfma16(f.a[m], f.b[u], acc[m][u]);
        else acc[m][u] = mfma16(f.b[u], f.a[m], acc[m][u]);
      }
    gram = mfma16(f.a[0], f.a[0], gram);
    rsum = mfma16(f.a[0], ones, rsum);
    if ((mw & 1) == ks) {
      if (mw < 2) {
#pragma unroll
        for (int i = 0; i < 4; ++i) csum[i] = mfma16(ones, f.b[i], csum[i]);
      } else {
#pragma unroll
        for (int i = 0; i < 3; ++i) csum[i] = mfma16(ones, f.b[4 + i], csum[i]);
      }
    }
  };

  if (w < 4) asm volatile("s_waitcnt vmcnt(12)" ::: "memory");
  else asm volatile("s_waitcnt vmcnt(10)" ::: "memory");
  __builtin_amdgcn_s_barrier();
  Frags f0, f1;
  load_frags(f0, 0, 0);
  unsigned slot_off = 0;
  for (int s = 0; s < 10; ++s) {
    load_frags(f1, slot_off, 1);
    if (s >= 1 && s <= 7) issue_half1(s + 2);
    mma(f0, 0);
    asm volatile("s_waitcnt lgkmcnt(0)" ::: "memory");
    if (s < 8) {
      if (w < 4) asm volatile("s_waitcnt vmcnt(6)" ::: "memory");
      else asm volatile("s_waitcnt vmcnt(5)" ::: "memory");
    } else {
      asm volatile("s_waitcnt vmcnt(0)" ::: "memory");
    }
    __builtin_amdgcn_s_barrier();
    __builtin_amdgcn_sched_barrier(0);
    slot_off += STAGE;
    if (slot_off == 3 * STAGE) slot_off = 0;
    if (s < 9) load_frags(f0, slot_off, 0);
    if (s <= 6) issue_half0(s + 3);
    if (s == 3 && has_task) {
      const int T = task / 10, c = task % 10;
      const unsigned so = L_SCR + w * 4096 + (4 * g + (r16 >> 2)) * 128 + 8 * (r16 & 3);
#pragma unroll
      for (int uu = 0; uu < 4; ++uu) {
        const bf16x8 fr = lds_tr2(smem, so + uu * 32, so + uu * 32 + 16 * 128);
        const int U = 4 * c + uu;
        *(u32x4*)(ws + WS_WF + (size_t)(((U / 5) * 68 + T) * 5 + (U % 5)) * 1024 + l * 16) =
            __builtin_bit_cast(u32x4, fr);
      }
    }
    mma(f1, 1);
  }

  LDS3 float* st_rs = (LDS3 float*)((LDS3 char*)smem + L_STAT);
  LDS3 float* st_rq = st_rs + 128;
  LDS3 float* st_cs = st_rs + 256;
  {
    const int rbase = 32 * mw + 16 * nw;
    if ((r16 >> 2) == g) {
      const int e = r16 & 3;
      const float q = e == 0 ? gram[0] : (e == 1 ? gram[1] : (e == 2 ? gram[2] : gram[3]));
      st_rq[rbase + r16] = q;
    }
    if (r16 == 0) *(LDS3 f32x4*)(st_rs + rbase + 4 * g) = rsum;
    if (g == 0) {
      const int ub = 7 * nw + (mw < 2 ? 0 : 4);
#pragma unroll
      for (int i = 0; i < 4; ++i)
        if (i < 3 || mw < 2) st_cs[(mw & 1) * 224 + (ub + i) * 16 + r16] = csum[i][0];
    }
  }
  __syncthreads();
  const float invD = 1.0f / 640.0f;
  float mu_l[2], rs_l[2];
#pragma unroll
  for (int m = 0; m < 2; ++m) {
    const int row = 32 * mw + 16 * (m ^ nw) + r16;
    const float mu = st_rs[row] * invD;
    const float var = fmaxf(st_rq[row] * invD - mu * mu, 0.f);
    mu_l[m] = mu;
    rs_l[m] = __builtin_amdgcn_rsqf(var + 1e-5f);
  }
  auto q_unit = [&](int u, int col0) {
    const int U = 7 * nw + u;
    const f32x4 c0 = *(const LDS3 f32x4*)(st_cs + U * 16 + 4 * g);
    const f32x4 c1 = *(const LDS3 f32x4*)(st_cs + 224 + U * 16 + 4 * g);
    const int Q = (col0 >> 2) + g;
    const int h = Q / 5, dq = Q % 5;
    const unsigned in = dq < 4 ? (dq >> 1) * 256 + r16 * 16 + (dq & 1) * 8 : 512 + r16 * 8;
#pragma unroll
    for (int m = 0; m < 2; ++m) {
      float v[4];
#pragma unroll
      for (int i = 0; i < 4; ++i) v[i] = phi_f(rs_l[m] * (acc[m][u][i] - mu_l[m] * (c0[i] + c1[i])));
      const int rt = rb * 8 + 2 * mw + (m ^ nw);
      *(u32x2*)(ws + WS_PQ + (size_t)(rt * 32 + h) * 640 + in) = u32x2{pk2(v[0], v[1]), pk2(v[2], v[3])};
    }
  };
  if (KV) {
    f32x4 mu4[2], rs4[2];
#pragma unroll
    for (int m = 0; m < 2; ++m) {
      const int row = 32 * mw + 16 * (m ^ nw) + 4 * g;
      const f32x4 a = *(const LDS3 f32x4*)(st_rs + row);
      const f32x4 b = *(const LDS3 f32x4*)(st_rq + row);
#pragma unroll
      for (int i = 0; i < 4; ++i) {
        const float mu = a[i] * invD;
        mu4[m][i] = mu;
        rs4[m][i] = __builtin_amdgcn_rsqf(fmaxf(b[i] * invD - mu * mu, 0.f) + 1e-5f);
      }
    }
#pragma unroll
    for (int u = 0; u < 5; ++u) {
      const int U = 7 * nw + u;
      const float cs = st_cs[U * 16 + r16] + st_cs[224 + U * 16 + r16];
      unsigned pkd[2][2];
#pragma unroll
      for (int m = 0; m < 2; ++m) {
        float v[4];
#pragma unroll
        for (int i = 0; i < 4; ++i) {
          const float t = rs4[m][i] * (acc[m][u][i] - mu4[m][i] * cs);
          v[i] = nw == 0 ? phi_f(t) : t;
        }
        pkd[m][0] = pk2(v[0], v[1]);
        pkd[m][1] = pk2(v[2], v[3]);
      }
      const u32x4 fr = nw == 0 ? u32x4{pkd[0][0], pkd[0][1], pkd[1][0], pkd[1][1]}
                               : u32x4{pkd[1][0], pkd[1][1], pkd[0][0], pkd[0][1]};
      *(LDS3 u32x4*)((LDS3 char*)smem + (mw * 10 + 5 * nw + u) * 1024 + l * 16) = fr;
    }
    q_unit(5, 64 * grp + 32 * nw);
    q_unit(6, 64 * grp + 32 * nw + 16);
    __syncthreads();
    for (int c = w; c < 18; c += 8) {
      int uk, uv;
      if (c < 13) {
        const int t = (c + 1) / 3;
        uk = t;
        uv = t + (c + 1) % 3 - 1;
        uv = ((c + 1) % 3 == 1) ? t : (((c + 1) % 3 == 2) ? t + 1 : t - 1);
      } else {
        uk = c - 13;
        uv = -1;
      }
      f32x4 o = {0.f, 0.f, 0.f, 0.f};
#pragma unroll
      for (int rg = 0; rg < 4; ++rg) {
        const bf16x8 ka = lds_b128(smem, (rg * 10 + uk) * 1024 + l * 16);
        bf16x8 vb = ones;
        if (uv >= 0) vb = lds_b128(smem, (rg * 10 + 5 + uv) * 1024 + l * 16);
        o = mfma16(ka, vb, o);
      }
      const int dcol = 16 * uk + 4 * g;
      const int hd = dcol / 20, dq = (dcol % 20) >> 2;
      int e = 20;
      bool ok = r16 == 0;
      if (uv >= 0) {
        const int ecol = 16 * uv + r16;
        e = ecol % 20;
        ok = (ecol / 20) == hd;
      }
      if (ok) *(f32x4*)(ws + WS_P + (size_t)((((4 * grp + hd) * 21 + e) * 5 + dq) * 16 + rb) * 16) = o;
    }
  } else {
    if (nw == 0) q_unit(0, 512 + 16 * grp);
#pragma unroll
    for (int pp = 0; pp < 3; ++pp) {
      f32x4 c[2];
#pragma unroll
      for (int hh = 0; hh < 2; ++hh) {
        const int U = 6 * nw + 1 + 2 * pp + hh;
        const f32x4 c0 = *(const LDS3 f32x4*)(st_cs + U * 16 + 4 * g);
        const f32x4 c1 = *(const LDS3 f32x4*)(st_cs + 224 + U * 16 + 4 * g);
        c[hh] = c0 + c1;
      }
#pragma unroll
      for (int m = 0; m < 2; ++m) {
        unsigned o[4];
#pragma unroll
        for (int hh = 0; hh < 2; ++hh) {
          const f32x4 a = nw == 0 ? acc[m][1 + 2 * pp + hh] : acc[m][2 * pp + hh];
          float v[4];
#pragma unroll
          for (int i = 0; i < 4; ++i) v[i] = fmaxf(rs_l[m] * (a[i] - mu_l[m] * c[hh][i]), 0.f);
          o[2 * hh] = pk2(v[0], v[1]);
          o[2 * hh + 1] = pk2(v[2], v[3]);
        }
        const int rt = rb * 8 + 2 * mw + (m ^ nw);
        const int kk = 6 * grp + 3 * nw + pp;
        *(u32x4*)(ws + WS_H + (size_t)(rt * 48 + kk) * 1024 + l * 16) = u32x4{o[0], o[1], o[2], o[3]};
      }
    }
  }
}

__global__ __launch_bounds__(512, 2) void k1_kernel(const char* __restrict__ x, const char* __restrict__ wqkv,
                                                    const char* __restrict__ w1, const char* __restrict__ wo,
                                                    const char* __restrict__ w2, char* __restrict__ ws) {
  extern __shared__ __attribute__((aligned(16))) char smem[];
  const int b = blockIdx.x, xcd = b & 7, j = b >> 3;
  const int rb = (xcd & 1) * 8 + (j & 7);
  const int ct = j >> 3;
  const int grp = (xcd >> 1) * 2 + (ct >> 1);
  const int rot = ((j & 7) + 3 * ct) % 10;
  if (ct & 1) k1_body<false>(x, wqkv, w1, wo, w2, ws, smem, rb, grp, rot);
  else k1_body<true>(x, wqkv, w1, wo, w2, ws, smem, rb, grp, rot);
}

template <int CTRL>
__device__ __forceinline__ float dpp_add(float v) {
  const int t = __builtin_amdgcn_update_dpp(0, __builtin_bit_cast(int, v), CTRL, 0xf, 0xf, true);
  return v + __builtin_bit_cast(float, t);
}
__device__ __forceinline__ float row16_sum(float v) {
  v = dpp_add<0x128>(v);
  v = dpp_add<0x124>(v);
  v = dpp_add<0x122>(v);
  v = dpp_add<0x121>(v);
  return v;
}
__device__ __forceinline__ bf16x8 ldg_frag(const char* p) {
  return __builtin_bit_cast(bf16x8, *(const u32x4*)p);
}

__global__ __launch_bounds__(256, 1) void k2_kernel(const char* __restrict__ x, char* __restrict__ ws,
                                                    char* __restrict__ out) {
  extern __shared__ __attribute__((aligned(16))) char smem[];
  const int tid = threadIdx.x;
  const int w = __builtin_amdgcn_readfirstlane(tid >> 6);
  const int l = tid & 63, r16 = l & 15, g = l >> 4;
  const int b = blockIdx.x, xcd = b & 7, j = b >> 3;
  const int rb = xcd * 4 + (j & 3), ct = j >> 2;

  const int dh = b >> 3, dp = b & 7;
  f32x4 part[4];
  if (w == 0) {
#pragma unroll
    for (int it = 0; it < 4; ++it) {
      const int item = it * 4 + g;
      int row = dp + 8 * (item / 5);
      if (row > 20) row = 20;
      part[it] = *(const f32x4*)(ws + WS_P + (size_t)(((dh * 21 + row) * 5 + item % 5) * 16 + r16) * 16);
    }
  }

  const char* hb = ws + WS_H + (size_t)((rb * 4) * 48 + 12 * w) * 1024 + l * 16;
  const char* wb = ws + WS_WF + (size_t)((ct * 4 + w) * 17) * 5120 + l * 16;
  constexpr int PD = 4;
  bf16x8 A[PD + 1][4], B[PD + 1][5];
  auto load_step = [&](int t) {
#pragma unroll
    for (int m = 0; m < 4; ++m) A[t % (PD + 1)][m] = ldg_frag(hb + m * 48 * 1024 + t * 1024);
#pragma unroll
    for (int u = 0; u < 5; ++u) B[t % (PD + 1)][u] = ldg_frag(wb + t * 5120 + u * 1024);
  };
#pragma unroll
  for (int t = 0; t < PD; ++t) load_step(t);
  __builtin_amdgcn_sched_barrier(0);

  if (w == 0) {
#pragma unroll
    for (int it = 0; it < 4; ++it) {
      const int item = it * 4 + g;
      const int row = dp + 8 * (item / 5), quad = item % 5;
      float s[4];
#pragma unroll
      for (int i = 0; i < 4; ++i) s[i] = row16_sum(part[it][i]);
      if (r16 == 0 && item < 15 && row <= 20) {
        GL1 unsigned long long* dst = (GL1 unsigned long long*)(ws + WS_F + dh * 1024 + row * 48 + quad * 8);
        const unsigned long long v = (unsigned long long)pk2(s[0], s[1]) | ((unsigned long long)pk2(s[2], s[3]) << 32);
        __hip_atomic_store(dst, v, __ATOMIC_RELAXED, __HIP_MEMORY_SCOPE_AGENT);
        if (quad == 4) __hip_atomic_store(dst + 1, 0ull, __ATOMIC_RELAXED, __HIP_MEMORY_SCOPE_AGENT);
      }
    }
    asm volatile("s_waitcnt vmcnt(0)" ::: "memory");
    if (l == 0)
      __hip_atomic_fetch_add((GL1 unsigned*)(ws + WS_CNT + dh * 128), 1u, __ATOMIC_RELAXED, __HIP_MEMORY_SCOPE_AGENT);
  }

  f32x4 acc[4][5];
#pragma unroll
  for (int m = 0; m < 4; ++m)
#pragma unroll
    for (int u = 0; u < 5; ++u) acc[m][u] = f32x4{0.f, 0.f, 0.f, 0.f};

  const unsigned pqoff = g < 2 ? g * 256 + r16 * 16 : (g == 2 ? 512 + r16 * 8 : r16 * 16);
  const char* pqb = ws + WS_PQ + (size_t)((rb * 4) * 32 + 8 * w) * 640 + pqoff;
  bf16x8 PQ[8][4], WO[5][5], FK[8][2];
  u32x2 xres[5];
  auto load_pq = [&](int h) {
#pragma unroll
    for (int m = 0; m < 4; ++m) {
      u32x4 v;
      __builtin_memcpy(&v, __builtin_assume_aligned(pqb + m * 32 * 640 + h * 640, 8), 16);
      PQ[h][m] = __builtin_bit_cast(bf16x8, v);
    }
  };
  auto load_wo = [&](int jj) {
#pragma unroll
    for (int u = 0; u < 5; ++u) WO[jj][u] = ldg_frag(wb + (12 + jj) * 5120 + u * 1024);
  };
  GL1 unsigned* cntp = (GL1 unsigned*)(ws + WS_CNT + (8 * w + (l & 7)) * 128);
  unsigned cnt0 = 0;

#pragma unroll
  for (int t = 0; t < 12; ++t) {
    if (t + PD < 12) load_step(t + PD);
    if (t == 8) { load_pq(0); load_pq(1); load_wo(0); cnt0 = __hip_atomic_load(cntp, __ATOMIC_RELAXED, __HIP_MEMORY_SCOPE_AGENT); }
    if (t == 9) { load_pq(2); load_pq(3); load_wo(1); }
    if (t == 10) {
      unsigned spins = 0;
      while (!__all(cnt0 == 8u)) {
        __builtin_amdgcn_s_sleep(2);
        cnt0 = __hip_atomic_load(cntp, __ATOMIC_RELAXED, __HIP_MEMORY_SCOPE_AGENT);
        if (++spins > (1u << 22)) break;
      }
      __builtin_amdgcn_fence(__ATOMIC_ACQUIRE, "wavefront");
      const auto rs = __builtin_amdgcn_make_buffer_rsrc((void*)(ws + WS_F), 0, 32 * 1024, 0x00020000);
      const int row0 = (r16 & 3) == 3 ? 20 : 5 * (r16 >> 2) + (r16 & 3);
      const int row1 = 5 * (r16 >> 2) + 3 + (r16 & 1);
      const unsigned o0 = g < 3 ? row0 * 48 + g * 16 : 0x40000000u;
      const unsigned o1 = g < 3 ? row1 * 48 + g * 16 : 0x40000000u;
#pragma unroll
      for (int h = 0; h < 8; ++h) {
        FK[h][0] = __builtin_bit_cast(bf16x8, __builtin_amdgcn_raw_buffer_load_b128(rs, o0, (8 * w + h) * 1024, 16));
        FK[h][1] = __builtin_bit_cast(bf16x8, __builtin_amdgcn_raw_buffer_load_b128(rs, o1, (8 * w + h) * 1024, 16));
      }
      load_pq(4); load_wo(2);
    }
    if (t == 11) {
      load_pq(5); load_pq(6); load_wo(3); load_pq(7); load_wo(4);
#pragma unroll
      for (int u = 0; u < 5; ++u)
        xres[u] = *(const u32x2*)(x + (size_t)(64 * rb + 16 * w + r16) * 1280 + (80 * ct + 16 * u + 4 * g) * 2);
    }
    __builtin_amdgcn_sched_barrier(0);
#pragma unroll
    for (int m = 0; m < 4; ++m)
#pragma unroll
      for (int u = 0; u < 5; ++u) acc[m][u] = mfma16(B[t % (PD + 1)][u], A[t % (PD + 1)][m], acc[m][u]);
    __builtin_amdgcn_sched_barrier(0);
  }

  float ov[4][40];
  auto head = [&](int h) {
#pragma unroll
    for (int m = 0; m < 4; ++m) {
      u32x4 q = __builtin_bit_cast(u32x4, PQ[h][m]);
      if (g == 2) { q[2] = 0u; q[3] = 0u; }
      const bf16x8 qb = __builtin_bit_cast(bf16x8, q);
      const f32x4 z = {0.f, 0.f, 0.f, 0.f};
      const f32x4 c0 = mfma16(FK[h][0], qb, z);
      const f32x4 c1 = mfma16(FK[h][1], qb, z);
      const float r = __builtin_amdgcn_rcpf(c0[3] + 1e-6f);
      ov[m][5 * h + 0] = c0[0] * r;
      ov[m][5 * h + 1] = c0[1] * r;
      ov[m][5 * h + 2] = c0[2] * r;
      ov[m][5 * h + 3] = c1[0] * r;
      ov[m][5 * h + 4] = c1[1] * r;
    }
  };
  auto ostep = [&](int jj) {
#pragma unroll
    for (int m = 0; m < 4; ++m) {
      const u32x4 p = {pk2(ov[m][8 * jj + 0], ov[m][8 * jj + 1]), pk2(ov[m][8 * jj + 2], ov[m][8 * jj + 3]),
                       pk2(ov[m][8 * jj + 4], ov[m][8 * jj + 5]), pk2(ov[m][8 * jj + 6], ov[m][8 * jj + 7])};
      const bf16x8 ob = __builtin_bit_cast(bf16x8, p);
#pragma unroll
      for (int u = 0; u < 5; ++u) acc[m][u] = mfma16(WO[jj][u], ob, acc[m][u]);
    }
  };
  head(0); head(1); ostep(0);
  head(2); head(3); ostep(1);
  head(4); ostep(2);
  head(5); head(6); ostep(3);
  head(7); ostep(4);

#pragma unroll
  for (int m = 0; m < 4; ++m) {
    if (m != w) {
      const int slot = w < m ? w : w - 1;
#pragma unroll
      for (int u = 0; u < 5; ++u)
        *(LDS3 f32x4*)((LDS3 char*)smem + ((m * 3 + slot) * 5 + u) * 1024 + l * 16) = acc[m][u];
    }
  }
  __syncthreads();
#pragma unroll
  for (int u = 0; u < 5; ++u) {
    f32x4 v = w == 0 ? acc[0][u] : (w == 1 ? acc[1][u] : (w == 2 ? acc[2][u] : acc[3][u]));
#pragma unroll
    for (int s = 0; s < 3; ++s) v += *(const LDS3 f32x4*)((LDS3 char*)smem + ((w * 3 + s) * 5 + u) * 1024 + l * 16);
    const float x0 = __builtin_bit_cast(float, xres[u][0] << 16), x1 = __builtin_bit_cast(float, xres[u][0] & 0xffff0000u);
    const float x2 = __builtin_bit_cast(float, xres[u][1] << 16), x3 = __builtin_bit_cast(float, xres[u][1] & 0xffff0000u);
    *(u32x2*)(out + (size_t)(64 * rb + 16 * w + r16) * 1280 + (80 * ct + 16 * u + 4 * g) * 2) =
        u32x2{pk2(v[0] + x0, v[1] + x1), pk2(v[2] + x2, v[3] + x3)};
  }
}

extern "C" void kernel_launch(void* const* d_in, const int* in_sizes, int n_in, void* d_out, int out_size, void* d_ws,
                              size_t ws_size, hipStream_t stream) {
  const char* x = (const char*)d_in[0];
  const char* wqkv = (const char*)d_in[1];
  const char* wo = (const char*)d_in[2];
  const char* w1 = (const char*)d_in[4];
  const char* w2 = (const char*)d_in[5];
  (void)hipFuncSetAttribute((const void*)k1_kernel, hipFuncAttributeMaxDynamicSharedMemorySize, K1_LDS);
  k1_kernel<<<256, 512, K1_LDS, stream>>>(x, wqkv, w1, wo, w2, (char*)d_ws);
  k2_kernel<<<256, 256, K2_LDS, stream>>>(x, (char*)d_ws, (char*)d_out);
}
```

```cpp
#include <hip/hip_runtime.h>
#include <hip/hip_bf16.h>
#include <stdint.h>

typedef __attribute__((ext_vector_type(8))) __bf16 bf16x8;
typedef __attribute__((ext_vector_type(4))) __bf16 bf16x4;
typedef __attribute__((ext_vector_type(2))) __bf16 bf16x2;
typedef __attribute__((ext_vector_type(4))) float f32x4;
typedef __attribute__((ext_vector_type(2))) float f32x2;
typedef __attribute__((ext_vector_type(4))) unsigned u32x4;
typedef __attribute__((ext_vector_type(2))) unsigned u32x2;
#define LDS3 __attribute__((address_space(3)))
#define GL1 __attribute__((address_space(1)))

constexpr unsigned WS_H = 0;
constexpr unsigned WS_PQ = WS_H + 128u * 48u * 1024u;
constexpr unsigned WS_WF = WS_PQ + 128u * 20u * 1024u;
constexpr unsigned WS_P = WS_WF + 8u * 68u * 5u * 1024u;
constexpr unsigned WS_F = WS_P + 32u * 21u * 5u * 16u * 16u;
constexpr unsigned F_BYTES = 32u * 840u;
constexpr unsigned WS_CNT = WS_F + 27u * 1024u;

constexpr int A_BYTES = 128 * 128;
constexpr int B_ROW = 448;
constexpr int B_BYTES = 64 * B_ROW;
constexpr int STAGE = A_BYTES + B_BYTES;
constexpr int L_SCR = 3 * STAGE;
constexpr int L_STAT = L_SCR + 3 * 4096;
constexpr int L_KV = 64 * 1024;
constexpr int K1_LDS = L_STAT + (256 + 448) * 4;
constexpr int L2_F = 4 * 20 * 1024;
constexpr int K2_LDS = L2_F + 27 * 1024;

__device__ __forceinline__ f32x4 mfma16(bf16x8 a, bf16x8 b, f32x4 c) {
  return __builtin_amdgcn_mfma_f32_16x16x32_bf16(a, b, c, 0, 0, 0);
}
__device__ __forceinline__ unsigned pk2(float a, float b) {
  f32x2 p = {a, b};
  return __builtin_bit_cast(unsigned, __builtin_convertvector(p, bf16x2));
}
template <bool V> struct BoolC { static constexpr bool value = V; };
#define SAUX 16
#define STORE16(off, v) __builtin_amdgcn_raw_buffer_store_b128((v), rws, (off), 0, SAUX)
#define QAUX 0
#define STORE8(off, v) __builtin_amdgcn_raw_buffer_store_b64((v), rws, (off), 0, QAUX)
#define STOREP(off, v) __builtin_amdgcn_raw_buffer_store_b128((v), rws, (off), 0, QAUX)
__device__ __forceinline__ void glds_v(const char* g, unsigned la, unsigned lb) {
  asm volatile("s_add_u32 m0, %1, %2\n\ts_nop 0\n\tglobal_load_lds_dwordx4 %0, off" ::"v"(g), "s"(la), "s"(lb) : "memory", "scc");
}
__device__ __forceinline__ void glds_s(unsigned vo, const char* sb, unsigned la, unsigned lb) {
  asm volatile("s_add_u32 m0, %2, %3\n\ts_nop 0\n\tglobal_load_lds_dwordx4 %0, %1" ::"v"(vo), "s"(sb), "s"(la), "s"(lb) : "memory", "scc");
}
__device__ __forceinline__ bf16x8 lds_b128(const char* smem, unsigned off) {
  return __builtin_bit_cast(bf16x8, *(const LDS3 u32x4*)((const LDS3 char*)smem + off));
}
__device__ __forceinline__ bf16x8 lds_tr2(const char* smem, unsigned off0, unsigned off1) {
  bf16x4 lo = __builtin_amdgcn_ds_read_tr16_b64_v4bf16((LDS3 bf16x4*)((LDS3 char*)smem + off0));
  bf16x4 hi = __builtin_amdgcn_ds_read_tr16_b64_v4bf16((LDS3 bf16x4*)((LDS3 char*)smem + off1));
  return __builtin_shufflevector(lo, hi, 0, 1, 2, 3, 4, 5, 6, 7);
}

struct Frags {
  bf16x8 a[4];
  bf16x8 b[7];
};
#define SB0() __builtin_amdgcn_sched_barrier(0)
#define LGKM0_BAR() do { asm volatile("s_waitcnt lgkmcnt(0)" ::: "memory"); __builtin_amdgcn_s_barrier(); asm volatile("" ::: "memory"); } while (0)

__device__ __forceinline__ float phi_e(float te) {
  const float e = __builtin_amdgcn_fmed3f(__builtin_amdgcn_exp2f(te * 1.44269504088896340736f), 0.f, 1.f);
  return fmaxf(te, 1e-6f) + e;
}
#define PFSLEEP 8
constexpr unsigned K2_CODE_BYTES = 13 * 1024;

template <int CTRL>
__device__ __forceinline__ float dpp_add(float v) {
  const int t = __builtin_amdgcn_update_dpp(0, __builtin_bit_cast(int, v), CTRL, 0xf, 0xf, true);
  return v + __builtin_bit_cast(float, t);
}

template <bool KV>
__device__ __forceinline__ void k1_body(const char* __restrict__ x, const char* __restrict__ wqkv,
                                        const char* __restrict__ w1, const char* __restrict__ wo,
                                        const char* __restrict__ w2, char* __restrict__ ws, char* smem, int rb, int grp,
                                        int rot) {
  const int tid = threadIdx.x;
  const int w = __builtin_amdgcn_readfirstlane(tid >> 6);
  const int l = tid & 63;
  const int r16 = l & 15, g = l >> 4;
  const unsigned lbase = __builtin_amdgcn_readfirstlane((unsigned)(uintptr_t)(LDS3 char*)smem);
  const auto rws = __builtin_amdgcn_make_buffer_rsrc((void*)ws, 0, WS_CNT, 0x00020000);
  LDS3 f32x2* st_row = (LDS3 f32x2*)((LDS3 char*)smem + L_STAT);
  LDS3 float* st_cs = (LDS3 float*)((LDS3 char*)smem + L_STAT + 1024);
  bf16x8 ones = __builtin_bit_cast(bf16x8, u32x4{0x3F803F80u, 0x3F803F80u, 0x3F803F80u, 0x3F803F80u});
  asm volatile("" : "+v"(ones));
  f32x4 ac[2][7];
  int mw, nw;

  if (w >= 4) {
    const int lw = w - 4;
    unsigned va[4];
#pragma unroll
    for (int i = 0; i < 4; ++i)
      va[i] = (unsigned)((rb * 128 + (lw + 4 * i) * 8 + (l >> 3)) * 1280 + (((l & 7) ^ (l >> 3)) * 16));
    unsigned vb[7];
    const char* pb[7];
#pragma unroll
    for (int i = 0; i < 7; ++i) {
      const int L = (lw + 4 * i) * 64 + l;
      const int row = (L * 2341) >> 16, cp = L - row * 28;
      const int c = cp ^ (((row >> 3) & 1) << 1);
      if (KV) {
        int col = 8 * c + 640 + 80 * grp;
        if (c >= 10) col = 8 * c + 64 * grp - 80;
        if (c >= 14) col = 8 * c + 1280 + 80 * grp - 112;
        if (c >= 24) col = 8 * c + 64 * grp + 32 - 192;
        vb[i] = (unsigned)(row * 3840 + col * 2);
      } else {
        const int cc = c < 26 ? c : c - 2;
        const unsigned oq = row * 3840 + (512 + 16 * grp + 8 * c) * 2;
        const unsigned oh = row * 3072 + (192 * grp + 8 * (cc - 2)) * 2;
        pb[i] = c < 2 ? wqkv + oq : w1 + oh;
        vb[i] = c < 2 ? 64 * 3840 : 64 * 3072;
      }
    }
    int kb = rot;
    unsigned slot = lbase + lw * 1024;
    f32x4 gram[2] = {}, rsum[2] = {};
    unsigned so_s = (32 * lw + r16) * 128 + ((g ^ (r16 & 7)) * 16);
    bf16x8 xs[4];
    auto stat_rd = [&]() {
#pragma unroll
      for (int i = 0; i < 4; ++i) xs[i] = lds_b128(smem, (so_s ^ ((i >> 1) * 64)) + (i & 1) * 2048);
      so_s = so_s >= 2 * STAGE ? so_s - 2 * STAGE : so_s + STAGE;
    };
    auto stat = [&](int i) {
      gram[i & 1] = mfma16(xs[i], xs[i], gram[i & 1]);
      rsum[i & 1] = mfma16(xs[i], ones, rsum[i & 1]);
      asm volatile("" : "+v"(gram[i & 1]), "+v"(rsum[i & 1]));
    };
    auto stage = [&](bool st) {
      const char* xa = x + kb * 128;
      const char* wb = wqkv + (unsigned)kb * (64 * 3840);
#pragma unroll
      for (int i = 0; i < 7; ++i) {
        if (i < 4) glds_s(va[i], xa, slot, i * 4096);
        if (KV) glds_s(vb[i], wb, slot, A_BYTES + i * 4096);
        else glds_v(pb[i] + (size_t)(unsigned)kb * vb[i], slot, A_BYTES + i * 4096);
        if (st && i >= 2 && i < 6) stat(i - 2);
        SB0();
      }
      kb = kb == 9 ? 0 : kb + 1;
      slot = slot >= lbase + 2 * STAGE ? slot - 2 * STAGE : slot + STAGE;
    };
    stage(false);
    stage(false);
    asm volatile("s_waitcnt vmcnt(11)" ::: "memory");
    __builtin_amdgcn_s_barrier();
#pragma unroll 1
    for (int s = 0; s < 8; ++s) {
      stat_rd();
      stage(true);
      asm volatile("s_waitcnt vmcnt(11)" ::: "memory");
      __builtin_amdgcn_s_barrier();
    }
    const int task = lw * 256 + blockIdx.x;
    const bool has_task = task < 680;
    const int T = task / 10, cl = task % 10;
    stat_rd();
    if (has_task) {
      const int w4 = T / 17, tt = T % 17;
#pragma unroll
      for (int pc = 0; pc < 4; ++pc) {
        const int rho = pc * 8 + (l >> 3);
        const char* src;
        if (tt < 12) {
          src = w2 + (unsigned)(((12 * w4 + tt) * 32 + rho) * 1280);
        } else {
          const int j = (rho >> 4) * 4 + (rho & 3), gg = (rho & 15) >> 2;
          const int L = 8 * (tt - 12) + j;
          const int hh = (L * 205) >> 10;
          src = wo + (unsigned)((160 * w4 + 15 * hh + 5 * gg + L) * 1280);
        }
        glds_v(src + cl * 128 + (l & 7) * 16, lbase + L_SCR + lw * 4096, pc * 1024);
      }
    } else if (lw == 3 && blockIdx.x < 27) {
      const unsigned off = blockIdx.x * 1024 + l * 16;
      const unsigned v = off < F_BYTES ? 0xffffffffu : 0u;
      STORE16(WS_F + off, (u32x4{v, v, v, v}));
    }
#pragma unroll
    for (int i = 0; i < 4; ++i) stat(i);
    if (has_task) asm volatile("s_waitcnt vmcnt(4)" ::: "memory");
    else asm volatile("s_waitcnt vmcnt(0)" ::: "memory");
    __builtin_amdgcn_s_barrier();
    stat_rd();
#pragma unroll
    for (int i = 0; i < 4; ++i) stat(i);
    __builtin_amdgcn_s_barrier();
#pragma unroll
    for (int m = 0; m < 2; ++m) {
      const int e = r16 & 3;
      const float a = e == 0 ? rsum[m][0] : (e == 1 ? rsum[m][1] : (e == 2 ? rsum[m][2] : rsum[m][3]));
      const float b = e == 0 ? gram[m][0] : (e == 1 ? gram[m][1] : (e == 2 ? gram[m][2] : gram[m][3]));
      const float mu = a * (1.0f / 640.0f);
      const float rs = __builtin_amdgcn_rsqf(fmaxf(b * (1.0f / 640.0f) - mu * mu, 0.f) + 1e-5f);
      if ((r16 >> 2) == g) st_row[32 * lw + 16 * m + r16] = f32x2{rs, mu * rs};
    }
    if (has_task) {
      asm volatile("s_waitcnt vmcnt(0)" ::: "memory");
      const unsigned so = L_SCR + lw * 4096 + (4 * g + (r16 >> 2)) * 128 + 8 * (r16 & 3);
#pragma unroll
      for (int uu = 0; uu < 4; ++uu) {
        const bf16x8 fr = lds_tr2(smem, so + uu * 32, so + uu * 32 + 16 * 128);
        const int U = 4 * cl + uu;
        STORE16(WS_WF + (unsigned)((((U / 5) * 68 + T) * 5 + (U % 5)) * 1024 + l * 16), __builtin_bit_cast(u32x4, fr));
      }
    }
    LGKM0_BAR();
    nw = (lw & 1) ^ 1;
    mw = 2 * (lw >> 1) + 1;
#pragma unroll
    for (int m = 0; m < 2; ++m)
#pragma unroll
      for (int u = 0; u < 7; ++u)
        ac[m][u] = *(const LDS3 f32x4*)((const LDS3 char*)smem + ((lw ^ 1) * 14 + m * 7 + u) * 1024 + l * 16);
  } else {
    const int mw2 = w >> 1;
    nw = w & 1;
    mw = 2 * mw2;
    const unsigned aoff = (64 * mw2 + r16) * 128 + ((g ^ (r16 & 7)) * 16);
    const unsigned brow = A_BYTES + (8 * g + (r16 >> 2)) * B_ROW + 8 * (r16 & 3) + nw * 224;
    const int flip = (g & 1) * 32;
    const unsigned bev = brow + (nw ? -flip : flip);
    const unsigned bod = brow + (nw ? flip : -flip);
    auto rd_a = [&](Frags& f, unsigned so, int ks, int m) { f.a[m] = lds_b128(smem, so + (aoff ^ (ks * 64)) + m * 2048); };
    auto rd_b = [&](Frags& f, unsigned so, int ks, int u) {
      const unsigned o = so + ((u & 1) ? bod : bev) + u * 32 + ks * 32 * B_ROW;
      f.b[u] = lds_tr2(smem, o, o + 4 * B_ROW);
    };
    int hselv = mw2;
    asm volatile("" : "+v"(hselv));
    const bool hsel = hselv != 0;
    f32x4 acc[4][7], cs[4];
#pragma unroll
    for (int u = 0; u < 7; ++u) {
      if (u < 4) cs[u] = f32x4{0.f, 0.f, 0.f, 0.f};
#pragma unroll
      for (int m = 0; m < 4; ++m) acc[m][u] = f32x4{0.f, 0.f, 0.f, 0.f};
    }
    auto half = [&](const Frags& fc, Frags& fn, unsigned so, int ksn) {
      u32x4 sel[4];
#pragma unroll
      for (int r = 0; r < 14; ++r) {
        const int u = r >> 1;
#pragma unroll
        for (int m = 2 * (r & 1); m < 2 * (r & 1) + 2; ++m) {
          if (KV && u < 5) acc[m][u] = mfma16(fc.a[m], fc.b[u], acc[m][u]);
          else acc[m][u] = mfma16(fc.b[u], fc.a[m], acc[m][u]);
        }
        if (r == 0) rd_b(fn, so, ksn, 0);
        if (r == 1 || r == 2) { rd_a(fn, so, ksn, 2 * r - 2); rd_a(fn, so, ksn, 2 * r - 1); }
        if (r >= 3 && r < 9) rd_b(fn, so, ksn, r - 2);
        if (r >= 9 && r < 13) {
          const u32x4 lo = __builtin_bit_cast(u32x4, fc.b[r - 9]), hi = __builtin_bit_cast(u32x4, fc.b[r - 6]);
          sel[r - 9] = u32x4{hsel ? hi[0] : lo[0], hsel ? hi[1] : lo[1], hsel ? hi[2] : lo[2], hsel ? hi[3] : lo[3]};
          asm volatile("" : "+v"(sel[r - 9]));
        }
        __builtin_amdgcn_sched_group_barrier(0x8, 1, 0);
        if (r < 9) __builtin_amdgcn_sched_group_barrier(0x100, 1, 0);
        else __builtin_amdgcn_sched_group_barrier(0x2, 2, 0);
        __builtin_amdgcn_sched_group_barrier(0x8, 1, 0);
        SB0();
      }
#pragma unroll
      for (int i = 0; i < 4; ++i) cs[i] = mfma16(ones, __builtin_bit_cast(bf16x8, sel[i]), cs[i]);
      SB0();
    };
    __builtin_amdgcn_s_barrier();
    asm volatile("" ::: "memory");
    Frags f0, f1;
#pragma unroll
    for (int m = 0; m < 4; ++m) rd_a(f0, 0, 0, m);
#pragma unroll
    for (int u = 0; u < 7; ++u) rd_b(f0, 0, 0, u);
    unsigned so_c = 0;
#pragma unroll 1
    for (int s = 0; s < 9; ++s) {
      half(f0, f1, so_c, 1);
      LGKM0_BAR();
      SB0();
      so_c = so_c == 2 * STAGE ? 0 : so_c + STAGE;
      half(f1, f0, so_c, 0);
    }
    half(f0, f1, so_c, 1);
    LGKM0_BAR();
    SB0();
#pragma unroll
    for (int mh = 1; mh >= 0; --mh) {
#pragma unroll
      for (int u = 0; u < 7; ++u) {
#pragma unroll
        for (int m = 2 * mh; m < 2 * mh + 2; ++m) {
          if (KV && u < 5) acc[m][u] = mfma16(f1.a[m], f1.b[u], acc[m][u]);
          else acc[m][u] = mfma16(f1.b[u], f1.a[m], acc[m][u]);
          if (mh == 0) *(LDS3 f32x4*)((LDS3 char*)smem + (w * 14 + m * 7 + u) * 1024 + l * 16) = acc[2 + m][u];
        }
        if (mh == 1 && u < 4) {
          const u32x4 lo = __builtin_bit_cast(u32x4, f1.b[u]), hi = __builtin_bit_cast(u32x4, f1.b[u + 3]);
          const u32x4 sel = {hsel ? hi[0] : lo[0], hsel ? hi[1] : lo[1], hsel ? hi[2] : lo[2], hsel ? hi[3] : lo[3]};
          cs[u] = mfma16(ones, __builtin_bit_cast(bf16x8, sel), cs[u]);
        }
        SB0();
      }
    }
    if (g == 0) {
#pragma unroll
      for (int i = 0; i < 4; ++i) st_cs[(7 * nw + 3 * mw2 + i) * 16 + r16] = cs[i][0];
    }
#pragma unroll
    for (int m = 0; m < 2; ++m)
#pragma unroll
      for (int u = 0; u < 7; ++u) ac[m][u] = acc[m][u];
    LGKM0_BAR();
  }

  f32x2 rl[2];
#pragma unroll
  for (int m = 0; m < 2; ++m) rl[m] = st_row[32 * mw + 16 * m + r16];
  const unsigned rt0 = rb * 8 + 2 * mw;
  auto q_vals = [&](int u, int m) {
    const f32x4 c = *(const LDS3 f32x4*)(st_cs + (7 * nw + u) * 16 + 4 * g);
    float v[4];
#pragma unroll
    for (int i = 0; i < 4; ++i) v[i] = phi_e(fmaf(rl[m][0], ac[m][u][i], fmaf(-rl[m][1], c[i], 1e-6f)));
    return u32x2{pk2(v[0], v[1]), pk2(v[2], v[3])};
  };
  auto q_pair = [&]() {
#pragma unroll
    for (int m = 0; m < 2; ++m) {
      const u32x2 a = q_vals(5, m), b = q_vals(6, m);
      STORE16(WS_PQ + (((rt0 + m) * 20 + 2 * grp + nw) * 1024 + l * 16), (u32x4{a[0], a[1], b[0], b[1]}));
    }
  };
  if (KV) {
    f32x2 r4[2][4];
#pragma unroll
    for (int m = 0; m < 2; ++m) {
      const LDS3 f32x4* p = (const LDS3 f32x4*)(st_row + 32 * mw + 16 * m + 4 * g);
      const f32x4 a = p[0], b = p[1];
      r4[m][0] = f32x2{a[0], a[1]}; r4[m][1] = f32x2{a[2], a[3]};
      r4[m][2] = f32x2{b[0], b[1]}; r4[m][3] = f32x2{b[2], b[3]};
    }
    auto kv_units = [&](auto isk) {
#pragma unroll
      for (int u = 0; u < 5; ++u) {
        const int U = 7 * nw + u;
        const float cs = st_cs[U * 16 + r16];
        unsigned pkd[4];
#pragma unroll
        for (int m = 0; m < 2; ++m) {
          float v[4];
#pragma unroll
          for (int i = 0; i < 4; ++i) {
            const float t = fmaf(r4[m][i][0], ac[m][u][i], fmaf(-r4[m][i][1], cs, isk.value ? 1e-6f : 0.f));
            v[i] = isk.value ? phi_e(t) : t;
          }
          pkd[2 * m] = pk2(v[0], v[1]);
          pkd[2 * m + 1] = pk2(v[2], v[3]);
        }
        *(LDS3 u32x4*)((LDS3 char*)smem + L_KV + (mw * 10 + 5 * nw + u) * 1024 + l * 16) = u32x4{pkd[0], pkd[1], pkd[2], pkd[3]};
      }
    };
    if (nw == 0) {
      kv_units(BoolC<true>{});
      LGKM0_BAR();
      q_pair();
    } else {
      kv_units(BoolC<false>{});
      q_pair();
      f32x4 o[5];
      int uks[5], uvs[5];
      unsigned poff[5];
#pragma unroll
      for (int bi = 0; bi < 5; ++bi) {
        const int c = mw + 4 * bi;
        const int t = ((c + 1) * 86) >> 8, rem = c + 1 - 3 * t;
        uks[bi] = c < 13 ? t : (c < 18 ? c - 13 : 0);
        uvs[bi] = c < 13 ? (rem == 1 ? t : (rem == 2 ? t + 1 : t - 1)) : -1;
        o[bi] = f32x4{0.f, 0.f, 0.f, 0.f};
        const int dq = 4 * uks[bi] + g;
        const int hd = (dq * 52) >> 8;
        const int ecol = 16 * uvs[bi] + r16;
        const int he = (ecol * 205) >> 12;
        const int e = uvs[bi] >= 0 ? ecol - 20 * he : 20;
        const bool ok = (uvs[bi] >= 0 ? he == hd : r16 == 0) && (c < 18);
        const unsigned off = WS_P + (unsigned)(((((4 * grp + hd) * 21 + e) * 5 + dq - 5 * hd) * 16 + rb) * 16);
        poff[bi] = ok ? off : 0x80000000u;
        asm volatile("" : "+v"(poff[bi]));
      }
      LGKM0_BAR();
#pragma unroll
      for (int rg = 0; rg < 4; ++rg)
#pragma unroll
        for (int bi = 0; bi < 5; ++bi) {
          const bf16x8 ka = lds_b128(smem, L_KV + (rg * 10 + uks[bi]) * 1024 + l * 16);
          bf16x8 vb = ones;
          if (bi < 3) vb = lds_b128(smem, L_KV + (rg * 10 + 5 + uvs[bi]) * 1024 + l * 16);
          if (bi == 3) {
            const bf16x8 t = lds_b128(smem, L_KV + (rg * 10 + 9) * 1024 + l * 16);
            vb = mw == 0 ? t : ones;
          }
          o[bi] = mfma16(ka, vb, o[bi]);
        }
#pragma unroll
      for (int bi = 0; bi < 5; ++bi) STOREP(poff[bi], __builtin_bit_cast(u32x4, o[bi]));
    }
  } else {
    auto h_units = [&](auto first) {
      constexpr int u0 = first.value ? 1 : 0;
#pragma unroll
      for (int pp = 0; pp < 3; ++pp) {
        f32x4 c[2];
#pragma unroll
        for (int hh = 0; hh < 2; ++hh) {
          const int U = 7 * nw + u0 + 2 * pp + hh;
          c[hh] = *(const LDS3 f32x4*)(st_cs + U * 16 + 4 * g);
        }
#pragma unroll
        for (int m = 0; m < 2; ++m) {
          unsigned o[4];
#pragma unroll
          for (int hh = 0; hh < 2; ++hh) {
            float v[4];
#pragma unroll
            for (int i = 0; i < 4; ++i)
              v[i] = fmaxf(fmaf(rl[m][0], ac[m][u0 + 2 * pp + hh][i], -rl[m][1] * c[hh][i]), 0.f);
            o[2 * hh] = pk2(v[0], v[1]);
            o[2 * hh + 1] = pk2(v[2], v[3]);
          }
          const int kk = 6 * grp + 3 * nw + pp;
          STORE16(WS_H + (((rt0 + m) * 48 + kk) * 1024 + l * 16), (u32x4{o[0], o[1], o[2], o[3]}));
        }
      }
    };
    if (nw == 0) {
      h_units(BoolC<true>{});
#pragma unroll
      for (int m = 0; m < 2; ++m)
        STORE8(WS_PQ + (((rt0 + m) * 20 + 16 + (grp >> 1)) * 1024 + l * 16 + (grp & 1) * 8), q_vals(0, m));
    } else {
      h_units(BoolC<false>{});
    }
  }
}

__global__ __launch_bounds__(512, 2) void k1_kernel(const char* __restrict__ x, const char* __restrict__ wqkv,
                                                    const char* __restrict__ w1, const char* __restrict__ wo,
                                                    const char* __restrict__ w2, char* __restrict__ ws) {
  extern __shared__ __attribute__((aligned(16))) char smem[];
  const int b = blockIdx.x, xcd = b & 7, j = b >> 3;
  const int rb = (xcd & 1) * 8 + (j & 7);
  const int ct = j >> 3;
  const int grp = (xcd >> 1) * 2 + (ct >> 1);
  const int rot = 0;
  unsigned pf = 0;
  {
    unsigned long long pc;
    unsigned span;
    asm volatile(".Lfa_k1_here:\n\ts_getpc_b64 %0\n\ts_mov_b32 %1, .Lfa_k2_begin-.Lfa_k1_here" : "=s"(pc), "=s"(span));
    const unsigned off = min((unsigned)((j % 6) * 8192 + (threadIdx.x & 63) * 128), span + K2_CODE_BYTES) & ~3u;
    if (threadIdx.x < 64)
      asm volatile("global_load_dword %0, %1, %2" : "=v"(pf) : "v"(off), "s"(pc & ~3ull) : "memory");
  }
  unsigned pd[2] = {0u, 0u};
  if (threadIdx.x < 256) {
    __builtin_amdgcn_s_sleep(PFSLEEP);
#pragma unroll
    for (int i = 0; i < 2; ++i) {
      const int c = __builtin_amdgcn_readfirstlane((threadIdx.x >> 6) * 2 + i + 8 * b);
      const char* src = c < 600 ? wqkv + c * 4096 : (c < 1080 ? w1 + (c - 600) * 4096 : x + (c - 1080) * 4096);
      if (c < 1720)
        asm volatile("global_load_dword %0, %1, %2" : "=v"(pd[i]) : "v"((threadIdx.x & 63) * 64), "s"(src) : "memory");
    }
  }
  if (ct & 1) k1_body<false>(x, wqkv, w1, wo, w2, ws, smem, rb, grp, rot);
  else k1_body<true>(x, wqkv, w1, wo, w2, ws, smem, rb, grp, rot);
  asm volatile("s_waitcnt vmcnt(0)" : "+v"(pd[0]), "+v"(pd[1]));
  asm volatile("s_waitcnt vmcnt(0)" : "+v"(pf));
}

#define HSLEEP 16
template <int NM, int NL>
__device__ __forceinline__ void interleave() {
#pragma unroll
  for (int i = 0; i < NL; ++i) {
    __builtin_amdgcn_sched_group_barrier(0x8, NM, 0);
    __builtin_amdgcn_sched_group_barrier(0x20, 1, 0);
  }
}

__global__ __launch_bounds__(320, 2) void k2_kernel(const char* __restrict__ x, char* __restrict__ ws,
                                                    char* __restrict__ out) {
  extern __shared__ __attribute__((aligned(16))) char smem[];
  unsigned long long pc2;
  asm volatile(".Lfa_k2_begin:\n\ts_getpc_b64 %0" : "=s"(pc2));
  const int tid = threadIdx.x;
  const int w = __builtin_amdgcn_readfirstlane(tid >> 6) - 1;
  const int l = tid & 63, r16 = l & 15, g = l >> 4;
  const int b = blockIdx.x, xcd = b & 7, j = b >> 3;
  const int rb = xcd * 4 + (j & 3), ct = j >> 2;
  const auto rw = __builtin_amdgcn_make_buffer_rsrc((void*)ws, 0, WS_CNT, 0x00020000);
  auto ld = [&](unsigned vo, unsigned so) {
    return __builtin_bit_cast(bf16x8, __builtin_amdgcn_raw_buffer_load_b128(rw, vo, so, 0));
  };
  const unsigned l16 = l * 16;

  if (w < 0) {
    __builtin_amdgcn_s_setprio(3);
    unsigned pf;
    asm volatile("global_load_dword %0, %1, %2" : "=v"(pf)
                 : "v"(min((unsigned)((j & 1) * 8192 + l * 128), K2_CODE_BYTES)), "s"(pc2 & ~3ull) : "memory");
    const int dh = b >> 3, dp = b & 7;
    const int item = l >> 2, ri = (item * 52) >> 8;
    const int row = dp + 8 * ri, quad = item - 5 * ri;
    const unsigned po = (((row > 20 ? 20 : row) * 5 + quad) * 16 + (l & 3)) * 16;
    f32x4 part[4];
#pragma unroll
    for (int it = 0; it < 4; ++it) part[it] = __builtin_bit_cast(f32x4, ld(po + it * 64, WS_P + dh * (21 * 5 * 256)));
    const f32x4 ps = (part[0] + part[1]) + (part[2] + part[3]);
    float s[4];
#pragma unroll
    for (int i = 0; i < 4; ++i) s[i] = dpp_add<0x4e>(dpp_add<0xb1>(ps[i]));
    if ((l & 3) == 0 && item < 15 && row <= 20) {
      GL1 unsigned long long* dst = (GL1 unsigned long long*)(ws + WS_F + dh * 840 + row * 40 + quad * 8);
      const unsigned long long v = (unsigned long long)pk2(s[0], s[1]) | ((unsigned long long)pk2(s[2], s[3]) << 32);
      __hip_atomic_store(dst, v, __ATOMIC_RELAXED, __HIP_MEMORY_SCOPE_AGENT);
    }
    u32x4 fb[27];
    __builtin_amdgcn_s_sleep(HSLEEP);
#pragma unroll
    for (int h = 0; h < 27; ++h) fb[h] = __builtin_amdgcn_raw_buffer_load_b128(rw, l16, WS_F + h * 1024, 16);
    for (unsigned spins = 0; spins < 20000u; ++spins) {
      bool again = false;
#pragma unroll
      for (int h = 0; h < 27; ++h) {
        if (__any(((fb[h][0] & fb[h][1]) == 0xffffffffu) | ((fb[h][2] & fb[h][3]) == 0xffffffffu))) {
          fb[h] = __builtin_amdgcn_raw_buffer_load_b128(rw, l16, WS_F + h * 1024, 16);
          again = true;
        }
      }
      if (!again) break;
    }
    asm volatile("s_waitcnt vmcnt(0)" : "+v"(pf));
#pragma unroll
    for (int h = 0; h < 27; ++h) *(LDS3 u32x4*)((LDS3 char*)smem + L2_F + h * 1024 + l16) = fb[h];
    LGKM0_BAR();
    __builtin_amdgcn_s_barrier();
    return;
  }

  const unsigned hb = WS_H + ((rb * 4) * 48 + 12 * w) * 1024;
  const unsigned wb = WS_WF + ((ct * 4 + w) * 17) * 5120;
  bf16x8 A[12][4], B[12][5];
  auto load_step = [&](int t) {
#pragma unroll
    for (int m = 0; m < 4; ++m) A[t][m] = ld(l16, hb + m * 48 * 1024 + t * 1024);
#pragma unroll
    for (int u = 0; u < 5; ++u) B[t][u] = ld(l16, wb + t * 5120 + u * 1024);
  };
  load_step(0);
  load_step(1);
  SB0();

  f32x4 acc[4][5];
#pragma unroll
  for (int m = 0; m < 4; ++m)
#pragma unroll
    for (int u = 0; u < 5; ++u) acc[m][u] = f32x4{0.f, 0.f, 0.f, 0.f};
  auto ffn = [&](int t) {
#pragma unroll
    for (int m = 0; m < 4; ++m)
#pragma unroll
      for (int u = 0; u < 5; ++u) acc[m][u] = mfma16(B[t][u], A[t][m], acc[m][u]);
  };

  const unsigned pqb = WS_PQ + ((rb * 4) * 20 + 5 * w) * 1024;
  bf16x8 PQ[5][4], WO[5][5];
  u32x2 xres[5];
  auto load_pq = [&](int t) {
#pragma unroll
    for (int m = 0; m < 4; ++m) PQ[t][m] = ld(l16, pqb + m * 20 * 1024 + t * 1024);
  };
  auto load_wo = [&](int jj) {
#pragma unroll
    for (int u = 0; u < 5; ++u) WO[jj][u] = ld(l16, wb + (12 + jj) * 5120 + u * 1024);
  };
  const int row0 = (r16 & 3) == 3 ? 20 : 5 * (r16 >> 2) + (r16 & 3);
  const int row1 = 5 * (r16 >> 2) + 3 + (r16 & 1);
  unsigned o0 = L2_F + w * (8 * 840) + row0 * 40 + g * 8;
  unsigned o1 = L2_F + w * (8 * 840) + row1 * 40 + g * 8;
  int gq = g;
  auto fq = [&](unsigned o, int h, int q0) {
    const int d0 = q0 - 5 * h;
    if (d0 > 4 || d0 + 3 < 0) return u32x2{0u, 0u};
    unsigned a = o + h * 840 + d0 * 8;
    if (d0 < 0) a = gq >= -d0 ? a : L2_F + F_BYTES;
    if (d0 > 1) a = gq <= 4 - d0 ? a : L2_F + F_BYTES;
    return *(const LDS3 u32x2*)((const LDS3 char*)smem + a);
  };
  auto ff = [&](unsigned o, int h, int t) {
    const u32x2 lo = fq(o, h, 8 * t), hi = fq(o, h, 8 * t + 4);
    return __builtin_bit_cast(bf16x8, u32x4{lo[0], lo[1], hi[0], hi[1]});
  };

  unsigned op[4][20];
  float carry[4];
  bf16x8 FF[8][4];
  auto head_rd = [&](int h) {
    const int t0 = (5 * h) >> 3, t1 = (5 * h + 4) >> 3;
    asm volatile("" : "+v"(o0), "+v"(o1), "+v"(gq));
    FF[h][0] = ff(o0, h, t0); FF[h][1] = ff(o1, h, t0);
    if (t1 != t0) { FF[h][2] = ff(o0, h, t1); FF[h][3] = ff(o1, h, t1); }
  };
  auto head = [&](int h) {
    const int t0 = (5 * h) >> 3, t1 = (5 * h + 4) >> 3;
    f32x4 ca[4], cb[4];
    {
      const bf16x8 f0 = FF[h][0], f1 = FF[h][1];
#pragma unroll
      for (int m = 0; m < 4; ++m) {
        const f32x4 z = {0.f, 0.f, 0.f, 0.f};
        ca[m] = mfma16(f0, PQ[t0][m], z);
        cb[m] = mfma16(f1, PQ[t0][m], z);
      }
    }
    if (t1 != t0) {
      const bf16x8 f0 = FF[h][2], f1 = FF[h][3];
#pragma unroll
      for (int m = 0; m < 4; ++m) {
        ca[m] = mfma16(f0, PQ[t1][m], ca[m]);
        cb[m] = mfma16(f1, PQ[t1][m], cb[m]);
      }
    }
#pragma unroll
    for (int m = 0; m < 4; ++m) {
      const f32x4 c0 = ca[m], c1 = cb[m];
      const float r = __builtin_amdgcn_rcpf(c0[3] + 1e-6f);
      if ((h & 1) == 0) {
        op[m][5 * (h >> 1) + 0] = pk2(c0[0] * r, c0[1] * r);
        op[m][5 * (h >> 1) + 1] = pk2(c0[2] * r, c1[0] * r);
        carry[m] = c1[1] * r;
      } else {
        op[m][5 * (h >> 1) + 2] = pk2(carry[m], c0[0] * r);
        op[m][5 * (h >> 1) + 3] = pk2(c0[1] * r, c0[2] * r);
        op[m][5 * (h >> 1) + 4] = pk2(c1[0] * r, c1[1] * r);
      }
    }
  };
  auto ostep = [&](int jj) {
#pragma unroll
    for (int m = 0; m < 4; ++m) {
      const u32x4 p = {op[m][4 * jj], op[m][4 * jj + 1], op[m][4 * jj + 2], op[m][4 * jj + 3]};
      const bf16x8 ob = __builtin_bit_cast(bf16x8, p);
#pragma unroll
      for (int u = 0; u < 5; ++u) acc[m][u] = mfma16(WO[jj][u], ob, acc[m][u]);
    }
  };

  load_step(2); ffn(0); interleave<2, 9>(); SB0();
  load_step(3); ffn(1); interleave<2, 9>(); SB0();
  load_step(4); ffn(2); interleave<2, 9>(); SB0();
  load_step(5); ffn(3); interleave<2, 9>(); SB0();
  load_step(6); ffn(4); interleave<2, 9>(); SB0();
  load_step(7); ffn(5); interleave<2, 9>(); SB0();
  load_step(8); ffn(6); interleave<2, 9>(); SB0();
  load_step(9); ffn(7); interleave<2, 9>(); SB0();
  load_pq(0); load_pq(1); ffn(8); interleave<2, 8>(); SB0();
  __builtin_amdgcn_s_barrier();
  asm volatile("" ::: "memory");
  SB0();
  head_rd(0); head_rd(1); load_wo(0); load_pq(2); ffn(9); interleave<2, 9>(); SB0();
  load_wo(1); head(0); head(1); head_rd(2); interleave<4, 5>(); SB0();
  load_pq(3); ostep(0); head(2); head_rd(3); interleave<6, 4>(); SB0();
  load_wo(2); head(3); head_rd(4); interleave<3, 5>(); SB0();
  load_pq(4); ostep(1); head(4); head_rd(5); interleave<8, 4>(); SB0();
  load_wo(3); load_wo(4); ostep(2); head(5); head_rd(6); interleave<2, 10>(); SB0();
  load_step(10); head(6); head_rd(7); interleave<1, 9>(); SB0();
  load_step(11); ostep(3); head(7); interleave<3, 9>(); SB0();
  int lz = l;
  asm volatile("" : "+v"(lz));
  const unsigned xo = (unsigned)((64 * rb + 16 * w + (lz & 15)) * 1280 + (80 * ct + 4 * (lz >> 4)) * 2);
#pragma unroll
  for (int u = 0; u < 5; ++u)
    xres[u] = *(const u32x2*)(x + (xo + 32 * u));
  ostep(4); ffn(10); interleave<4, 5>(); SB0();
#pragma unroll
  for (int m = 0; m < 4; ++m) {
#pragma unroll
    for (int u = 0; u < 5; ++u) acc[m][u] = mfma16(B[11][u], A[11][m], acc[m][u]);
    if (m > 0) {
#pragma unroll
      for (int u = 0; u < 5; ++u)
        *(LDS3 f32x4*)((LDS3 char*)smem + (((m - 1) * 4 + w) * 5 + u) * 1024 + l * 16) = acc[m - 1][u];
    }
    SB0();
  }
#pragma unroll
  for (int u = 0; u < 5; ++u) *(LDS3 f32x4*)((LDS3 char*)smem + ((12 + w) * 5 + u) * 1024 + l * 16) = acc[3][u];
  LGKM0_BAR();
#pragma unroll
  for (int u = 0; u < 5; ++u) {
    const LDS3 char* pp = (const LDS3 char*)smem + (w * 20 + u) * 1024 + l * 16;
    const f32x4 v = (*(const LDS3 f32x4*)pp + *(const LDS3 f32x4*)(pp + 5120)) +
                    (*(const LDS3 f32x4*)(pp + 10240) + *(const LDS3 f32x4*)(pp + 15360));
    const float x0 = __builtin_bit_cast(float, xres[u][0] << 16), x1 = __builtin_bit_cast(float, xres[u][0] & 0xffff0000u);
    const float x2 = __builtin_bit_cast(float, xres[u][1] << 16), x3 = __builtin_bit_cast(float, xres[u][1] & 0xffff0000u);
    *(u32x2*)(out + (xo + 32 * u)) =
        u32x2{pk2(v[0] + x0, v[1] + x1), pk2(v[2] + x2, v[3] + x3)};
  }
}

extern "C" void kernel_launch(void* const* d_in, const int* in_sizes, int n_in, void* d_out, int out_size, void* d_ws,
                              size_t ws_size, hipStream_t stream) {
  const char* x = (const char*)d_in[0];
  const char* wqkv = (const char*)d_in[1];
  const char* wo = (const char*)d_in[2];
  const char* w1 = (const char*)d_in[4];
  const char* w2 = (const char*)d_in[5];
  (void)hipFuncSetAttribute((const void*)k1_kernel, hipFuncAttributeMaxDynamicSharedMemorySize, K1_LDS);
  (void)hipFuncSetAttribute((const void*)k2_kernel, hipFuncAttributeMaxDynamicSharedMemorySize, K2_LDS);
  k1_kernel<<<256, 512, K1_LDS, stream>>>(x, wqkv, w1, wo, w2, (char*)d_ws);
  k2_kernel<<<256, 320, K2_LDS, stream>>>(x, (char*)d_ws, (char*)d_out);
}
```
